```python
import torch

def _softmax(x, axis=-1):
    return torch.softmax(x, dim=axis)
def _gelu(x):
    return torch.nn.functional.gelu(x, approximate="tanh")
def _silu(x):
    return torch.nn.functional.silu(x)
def _sigmoid(x):
    return torch.sigmoid(x)
def _layernorm(x, axis=-1, eps=1e-5):
    m = x.mean(dim=axis, keepdim=True)
    v = x.var(dim=axis, keepdim=True, unbiased=False)
    return (x - m) / torch.sqrt(v + eps)
def _scatter_add(tgt, idx, src, axis=0):
    return tgt.clone().index_add_(axis, idx, src)
def _segment_sum(src, seg, n):
    out = torch.zeros((n,) + src.shape[1:], dtype=src.dtype)
    return out.index_add_(0, seg, src)
def _pad(x, pw):
    flat = tuple(v for pair in reversed(pw) for v in pair)
    return torch.nn.functional.pad(x, flat)

kernel_name = 'parallel_sum__mha_block_cached__glu_gated_bilinear__decode'

def setup_inputs():

    _w = (lambda a: (lambda t: t.to(torch.bfloat16) if t.is_floating_point() else t)(torch.as_tensor(a)))
    def _A_setup():
        B, Sq, D = 1, 1, 576
        H, Sk, Dh = 32, 16384, 128
        return {
            "x": _w((lambda *s: torch.randn(*s, dtype=torch.bfloat16) * 0.05)(B, Sq, D)),
            "k_cache": _w((lambda *s: torch.randn(*s, dtype=torch.bfloat16) * 3.2)(B, H, Sk, Dh)),
            "v_cache": _w((lambda *s: torch.randn(*s, dtype=torch.bfloat16) * 1.6)(B, H, Sk, Dh)),
            "wq": _w((lambda *s: torch.randn(*s, dtype=torch.bfloat16) * 3.2)(D, H * Dh) * (D ** -0.5)),
            "wo": _w((lambda *s: torch.randn(*s, dtype=torch.bfloat16) * 1.6)(H * Dh, D) * ((H * Dh) ** -0.5)),
        }
    _A_in = _A_setup()

    def _B_setup():
        B, S, D, F = 1, 1, 576, 4608
        return {
            "x": _w((lambda *s: torch.randn(*s, dtype=torch.bfloat16) * 0.05)(B, S, D)),
            "w_gate": _w((lambda *s: torch.randn(*s, dtype=torch.bfloat16) * 1.6)(D, F) * (D ** -0.5)),
            "w_up": _w((lambda *s: torch.randn(*s, dtype=torch.bfloat16) * 1.6)(D, F) * (D ** -0.5)),
            "w_down": _w((lambda *s: torch.randn(*s, dtype=torch.bfloat16) * 1.6)(F, D) * (F ** -0.5)),
        }
    _B_in = _B_setup()

    return {
        "A_x": _A_in["x"],
        "A_k_cache": _A_in["k_cache"],
        "A_v_cache": _A_in["v_cache"],
        "A_wq": _A_in["wq"],
        "A_wo": _A_in["wo"],
        "B_x": _B_in["x"],
        "B_w_gate": _B_in["w_gate"],
        "B_w_up": _B_in["w_up"],
        "B_w_down": _B_in["w_down"],
    }

def reference(A_x, A_k_cache, A_v_cache, A_wq, A_wo, B_x, B_w_gate, B_w_up, B_w_down):
    def _A_ref(x, k_cache, v_cache, wq, wo):
        B, Sq, D = x.shape
        H, Dh = 32, 128
        q = (lambda x, *d: x.permute(*d))((x @ wq).reshape(B, Sq, H, Dh), 0, 2, 1, 3)
        scores = torch.einsum("bhqd,bhkd->bhqk", q, k_cache) * (Dh ** -0.5)
        p = _softmax(scores, axis=-1)
        o = torch.einsum("bhqk,bhkd->bhqd", p, v_cache)
        return (lambda x, *d: x.permute(*d))(o, 0, 2, 1, 3).reshape(B, Sq, H * Dh) @ wo

    def _B_ref(x, w_gate, w_up, w_down):
        g = torch.einsum("bsd,df->bsf", x, w_gate)
        u = torch.einsum("bsd,df->bsf", x, w_up)
        h = g * u
        return torch.einsum("bsf,fd->bsd", h, w_down)

    x = A_x
    xn = _layernorm(x, axis=-1)
    return x + _A_ref(xn, A_k_cache, A_v_cache, A_wq, A_wo) + _B_ref(xn, B_w_gate, B_w_up, B_w_down)
```

```cpp
#include <hip/hip_runtime.h>
#include <hip/hip_bf16.h>
#include <stdint.h>

#define MIMIC 0

typedef unsigned u32x4 __attribute__((ext_vector_type(4)));
typedef __bf16 bf16x2 __attribute__((ext_vector_type(2)));
typedef unsigned short u16;

constexpr int D = 576, H = 32, SK = 16384, DH = 128, F = 4608;
constexpr int NSPLIT = 8, NW = 8, NT = NW * 64;
constexpr int NFFN = 72;

__device__ __forceinline__ float bf2f(u16 v) { return __uint_as_float(((unsigned)v) << 16); }
__device__ __forceinline__ float lo2f(unsigned v) { return __uint_as_float(v << 16); }
__device__ __forceinline__ float hi2f(unsigned v) { return __uint_as_float(v & 0xffff0000u); }
__device__ __forceinline__ unsigned cvtpk(float lo, float hi) {
  unsigned r; asm("v_cvt_pk_bf16_f32 %0, %1, %2" : "=v"(r) : "v"(lo), "v"(hi)); return r;
}
__device__ __forceinline__ float rbf(float x) { return __uint_as_float(cvtpk(x, x) << 16); }
__device__ __forceinline__ float mrbf(float x) { return MIMIC ? rbf(x) : x; }
__device__ __forceinline__ float dot2(unsigned k, unsigned q, float acc) {
  return __builtin_amdgcn_fdot2_f32_bf16(__builtin_bit_cast(bf16x2, k), __builtin_bit_cast(bf16x2, q), acc, false);
}
__device__ __forceinline__ float wsum(float v) {
  for (int o = 32; o > 0; o >>= 1) v += __shfl_xor(v, o);
  return v;
}
__device__ __forceinline__ float wmax(float v) {
  for (int o = 32; o > 0; o >>= 1) v = fmaxf(v, __shfl_xor(v, o));
  return v;
}

__device__ __forceinline__ void layernorm_lds(const u16* x, float* xn, int tid) {
  int lane = tid & 63;
  float xv[9]; float s = 0.f;
#pragma unroll
  for (int i = 0; i < 9; ++i) { xv[i] = bf2f(x[lane + 64 * i]); s += xv[i]; }
  float mean = wsum(s) * (1.f / D);
  float ss = 0.f;
#pragma unroll
  for (int i = 0; i < 9; ++i) { float d = xv[i] - mean; ss += d * d; }
  float var = wsum(ss) * (1.f / D);
  float mb = mrbf(mean), vb = mrbf(var);
  float den = mrbf(sqrtf(mrbf(vb + 1e-5f)));
  if (tid < 64) {
#pragma unroll
    for (int i = 0; i < 9; ++i) xn[lane + 64 * i] = rbf(mrbf(xv[i] - mb) / den);
  }
}

__global__ void __launch_bounds__(NT) k_main(const u16* __restrict__ x, const u16* __restrict__ kc, const u16* __restrict__ vc,
    const u16* __restrict__ wq, const u16* __restrict__ wg, const u16* __restrict__ wu, const u16* __restrict__ wd,
    float* __restrict__ part, float* __restrict__ yffn) {
  __shared__ float xn[D];
  __shared__ float red[NW][2 * DH + 8];
  __shared__ unsigned qpk[DH / 2];
  const int tid = threadIdx.x, lane = tid & 63, w = tid >> 6;
  const int r = lane >> 4, c = lane & 15;
  layernorm_lds(x, xn, tid);
  __syncthreads();
  const int b = blockIdx.x;
  if (b >= 256) {
    const int f0 = (b - 256) * 64;
    const int rr = lane >> 3, cc = lane & 7;
    float g[8], u[8];
#pragma unroll
    for (int i = 0; i < 8; ++i) { g[i] = 0.f; u[i] = 0.f; }
#pragma unroll
    for (int it = 0; it < 9; ++it) {
      int row = w * 72 + it * 8 + rr;
      u32x4 a = *(const u32x4*)(wg + (size_t)row * F + f0 + cc * 8);
      u32x4 bb = *(const u32x4*)(wu + (size_t)row * F + f0 + cc * 8);
      float xv = xn[row];
#pragma unroll
      for (int k = 0; k < 4; ++k) {
        g[2 * k] += xv * lo2f(a[k]); g[2 * k + 1] += xv * hi2f(a[k]);
        u[2 * k] += xv * lo2f(bb[k]); u[2 * k + 1] += xv * hi2f(bb[k]);
      }
    }
#pragma unroll
    for (int i = 0; i < 8; ++i) {
      for (int o = 8; o < 64; o <<= 1) { g[i] += __shfl_xor(g[i], o); u[i] += __shfl_xor(u[i], o); }
    }
    if (rr == 0) {
#pragma unroll
      for (int i = 0; i < 8; ++i) { red[w][cc * 8 + i] = g[i]; red[w][64 + cc * 8 + i] = u[i]; }
    }
    __syncthreads();
    __shared__ float hs[64];
    if (tid < 64) {
      float gg = 0.f, uu = 0.f;
      for (int ww = 0; ww < NW; ++ww) { gg += red[ww][tid]; uu += red[ww][64 + tid]; }
      hs[tid] = mrbf(mrbf(gg) * mrbf(uu));
    }
    __syncthreads();
    if (tid < 288) {
      float a0 = 0.f, a1 = 0.f;
#pragma unroll 16
      for (int i = 0; i < 64; ++i) {
        unsigned v = *(const unsigned*)(wd + (size_t)(f0 + i) * D + tid * 2);
        a0 += hs[i] * lo2f(v); a1 += hs[i] * hi2f(v);
      }
      yffn[(b - 256) * D + tid * 2] = a0; yffn[(b - 256) * D + tid * 2 + 1] = a1;
    }
    return;
  }
  const int g8 = b & 7, j = b >> 3;
  const int h = g8 * 4 + (j >> 3), s = j & 7;
  {
    float acc[8];
#pragma unroll
    for (int i = 0; i < 8; ++i) acc[i] = 0.f;
#pragma unroll
    for (int it = 0; it < 18; ++it) {
      int row = w * 72 + it * 4 + r;
      u32x4 a = *(const u32x4*)(wq + (size_t)row * (H * DH) + h * DH + c * 8);
      float xv = xn[row];
#pragma unroll
      for (int k = 0; k < 4; ++k) { acc[2 * k] += xv * lo2f(a[k]); acc[2 * k + 1] += xv * hi2f(a[k]); }
    }
#pragma unroll
    for (int i = 0; i < 8; ++i) { acc[i] += __shfl_xor(acc[i], 16); acc[i] += __shfl_xor(acc[i], 32); }
    if (r == 0) {
#pragma unroll
      for (int i = 0; i < 8; ++i) red[w][c * 8 + i] = acc[i];
    }
    __syncthreads();
    if (tid < 64) {
      float q0 = 0.f, q1 = 0.f;
      for (int ww = 0; ww < NW; ++ww) { q0 += red[ww][2 * tid]; q1 += red[ww][2 * tid + 1]; }
      qpk[tid] = cvtpk(q0, q1);
    }
    __syncthreads();
  }
  u32x4 q = *(const u32x4*)&qpk[c * 4];
  const size_t base = ((size_t)h * SK + s * (SK / NSPLIT) + w * 256) * DH + (size_t)lane * 8;
  const u16* kp = kc + base; const u16* vp = vc + base;
  float m = -INFINITY, l = 0.f, o[8];
#pragma unroll
  for (int i = 0; i < 8; ++i) o[i] = 0.f;
  const float scale = 0.08838834764831845f, L2E = 1.4426950408889634f;
  for (int t0 = 0; t0 < 16; ++t0) {
    u32x4 K[4], V[4];
#pragma unroll
    for (int t = 0; t < 4; ++t) {
      K[t] = __builtin_nontemporal_load((const u32x4*)(kp + (t0 * 4 + t) * 512));
      V[t] = __builtin_nontemporal_load((const u32x4*)(vp + (t0 * 4 + t) * 512));
    }
    float sc[4]; float mx = m;
#pragma unroll
    for (int t = 0; t < 4; ++t) {
      float d = 0.f;
#pragma unroll
      for (int k = 0; k < 4; ++k) d = dot2(K[t][k], q[k], d);
      for (int of = 1; of < 16; of <<= 1) d += __shfl_xor(d, of);
      sc[t] = mrbf(mrbf(d) * scale) * L2E;
      mx = fmaxf(mx, sc[t]);
    }
    mx = fmaxf(mx, __shfl_xor(mx, 16)); mx = fmaxf(mx, __shfl_xor(mx, 32));
    float al = exp2f(m - mx); m = mx;
    l *= al;
#pragma unroll
    for (int i = 0; i < 8; ++i) o[i] *= al;
#pragma unroll
    for (int t = 0; t < 4; ++t) {
      float p = exp2f(sc[t] - m);
      l += p;
#pragma unroll
      for (int k = 0; k < 4; ++k) { o[2 * k] += p * lo2f(V[t][k]); o[2 * k + 1] += p * hi2f(V[t][k]); }
    }
  }
  l += __shfl_xor(l, 16); l += __shfl_xor(l, 32);
#pragma unroll
  for (int i = 0; i < 8; ++i) { o[i] += __shfl_xor(o[i], 16); o[i] += __shfl_xor(o[i], 32); }
  __syncthreads();
  if (r == 0) {
#pragma unroll
    for (int i = 0; i < 8; ++i) red[w][c * 8 + i] = o[i];
    if (c == 0) { red[w][DH] = m; red[w][DH + 1] = l; }
  }
  __syncthreads();
  if (tid < DH) {
    float M = -INFINITY;
    for (int ww = 0; ww < NW; ++ww) M = fmaxf(M, red[ww][DH]);
    float L = 0.f, O = 0.f;
    for (int ww = 0; ww < NW; ++ww) { float e = exp2f(red[ww][DH] - M); L += e * red[ww][DH + 1]; O += e * red[ww][tid]; }
    float* pp = part + (size_t)(h * NSPLIT + s) * 132;
    pp[tid] = O;
    if (tid == 0) { pp[DH] = M; pp[DH + 1] = L; }
  }
}

__global__ void __launch_bounds__(320) k_comb(const float* __restrict__ part, const u16* __restrict__ wo, float* __restrict__ yatt) {
  __shared__ float os[DH];
  const int h = blockIdx.x, tid = threadIdx.x;
  if (tid < DH) {
    const float* pp = part + (size_t)h * NSPLIT * 132;
    float M = -INFINITY;
    for (int s = 0; s < NSPLIT; ++s) M = fmaxf(M, pp[s * 132 + DH]);
    float L = 0.f, O = 0.f;
    for (int s = 0; s < NSPLIT; ++s) { float e = exp2f(pp[s * 132 + DH] - M); L += e * pp[s * 132 + DH + 1]; O += e * pp[s * 132 + tid]; }
    os[tid] = mrbf(O / L);
  }
  __syncthreads();
  if (tid < 288) {
    float a0 = 0.f, a1 = 0.f;
#pragma unroll 16
    for (int i = 0; i < DH; ++i) {
      unsigned v = *(const unsigned*)(wo + (size_t)(h * DH + i) * D + tid * 2);
      a0 += os[i] * lo2f(v); a1 += os[i] * hi2f(v);
    }
    yatt[h * D + tid * 2] = a0; yatt[h * D + tid * 2 + 1] = a1;
  }
}

__global__ void __launch_bounds__(64) k_fin(const u16* __restrict__ x, const float* __restrict__ yatt, const float* __restrict__ yffn, u16* __restrict__ out) {
  const int col = blockIdx.x * 64 + threadIdx.x;
  float a = 0.f, bsum = 0.f;
#pragma unroll
  for (int h = 0; h < H; ++h) a += yatt[h * D + col];
#pragma unroll
  for (int i = 0; i < NFFN; ++i) bsum += yffn[i * D + col];
  float res = mrbf(bf2f(x[col]) + mrbf(a)) + mrbf(bsum);
  out[col] = (u16)(cvtpk(res, res) & 0xffff);
}

extern "C" void kernel_launch(void* const* d_in, const int* in_sizes, int n_in, void* d_out, int out_size, void* d_ws, size_t ws_size, hipStream_t stream) {
  const u16* x = (const u16*)d_in[0]; const u16* kc = (const u16*)d_in[1]; const u16* vc = (const u16*)d_in[2];
  const u16* wq = (const u16*)d_in[3]; const u16* wo = (const u16*)d_in[4];
  const u16* wg = (const u16*)d_in[6]; const u16* wu = (const u16*)d_in[7]; const u16* wd = (const u16*)d_in[8];
  float* part = (float*)d_ws; float* yffn = part + 256 * 132; float* yatt = yffn + NFFN * D;
  k_main<<<256 + NFFN, NT, 0, stream>>>(x, kc, vc, wq, wg, wu, wd, part, yffn);
  k_comb<<<H, 320, 0, stream>>>(part, wo, yatt);
  k_fin<<<D / 64, 64, 0, stream>>>(x, yatt, yffn, (u16*)d_out);
}
```

```cpp
#include <hip/hip_runtime.h>
#include <hip/hip_bf16.h>
#include <stdint.h>

constexpr int D1 = 5;
constexpr int D2 = 6;
constexpr int DR = 7;
constexpr int DL = 8;
constexpr int T2 = 11;
constexpr int PRE = 56;
constexpr int FD = 12;
constexpr int SD = 4;
constexpr int PACE_T4 = 1120, PACE_1 = 232, PACE_2 = 188;
constexpr int Q_DUE = 650;

typedef unsigned u32;
typedef unsigned long long u64;
typedef unsigned short u16;
typedef u32 u32x4 __attribute__((ext_vector_type(4)));
typedef u32 u32x2 __attribute__((ext_vector_type(2)));
typedef float f32x4 __attribute__((ext_vector_type(4)));
typedef float f32x2 __attribute__((ext_vector_type(2)));
typedef __bf16 bf16x2 __attribute__((ext_vector_type(2)));
typedef __bf16 bf16x8 __attribute__((ext_vector_type(8)));
#define GAS __attribute__((address_space(1)))
#define LAS __attribute__((address_space(3)))
typedef GAS u32 gu32;
typedef GAS u64 gu64;
#define RLX_AGENT __ATOMIC_RELAXED, __HIP_MEMORY_SCOPE_AGENT
#define RLX_WG __ATOMIC_RELAXED, __HIP_MEMORY_SCOPE_WORKGROUP

constexpr int D = 576, H = 32, DH = 128, F = 4608;
constexpr int NWG = 256, NRED = 72;
constexpr int NATT = 7;
constexpr u32 SPIN_CAP = 1u << 18;

constexpr int WS_GO = 0;
constexpr int WS_HB = 256;
constexpr int WS_Q = WS_HB + H * 64;
constexpr int WS_PO = WS_Q + H * 64 * 8;
constexpr int WS_ML = WS_PO + NWG * 64 * 8;
constexpr int WS_YA = WS_ML + NWG * 64;
constexpr int WS_END = WS_YA + NRED * H * 64;

constexpr int L_XN = 0;
constexpr int L_AO = 1152;
constexpr int L_AM = L_AO + 4 * 512;
constexpr int L_FL = L_AM + 64;
constexpr int L_HB = L_FL + 32;
constexpr int L_YF = L_HB + 768;
constexpr int L_Q = L_YF;
constexpr int L_WO = L_YF + 3 * 2304;
constexpr int L_END = L_WO + 128 * 1152;

__device__ __forceinline__ float lo2f(u32 v) { return __uint_as_float(v << 16); }
__device__ __forceinline__ float hi2f(u32 v) { return __uint_as_float(v & 0xffff0000u); }
__device__ __forceinline__ u32 cvtpk(float lo, float hi) {
  bf16x2 r = {(__bf16)lo, (__bf16)hi};
  return __builtin_bit_cast(u32, r);
}
__device__ __forceinline__ float rbf(float x) { return (float)(__bf16)x; }
__device__ __forceinline__ float dot2(u32 a, u32 b, float acc) {
  return __builtin_amdgcn_fdot2_f32_bf16(__builtin_bit_cast(bf16x2, a), __builtin_bit_cast(bf16x2, b), acc, false);
}
template <int CTRL> __device__ __forceinline__ u32 dppu(u32 x) { return (u32)__builtin_amdgcn_mov_dpp((int)x, CTRL, 0xf, 0xf, true); }
template <int CTRL> __device__ __forceinline__ float dppf(float x) { return __uint_as_float(dppu<CTRL>(__float_as_uint(x))); }
constexpr int XOR1 = 0xB1, XOR2 = 0x4E, XOR7 = 0x141, XOR8 = 0x128, BCAST = 0x150;
__device__ __forceinline__ float fold32(float a, float b) {
  auto s = __builtin_amdgcn_permlane32_swap(__float_as_uint(a), __float_as_uint(b), false, false);
  return __uint_as_float(s[0]) + __uint_as_float(s[1]);
}
__device__ __forceinline__ float fold16(float a, float b) {
  auto s = __builtin_amdgcn_permlane16_swap(__float_as_uint(a), __float_as_uint(b), false, false);
  return __uint_as_float(s[0]) + __uint_as_float(s[1]);
}
__device__ __forceinline__ float wave_sum(float x) {
  x += dppf<XOR1>(x); x += dppf<XOR2>(x); x += dppf<XOR7>(x); x += dppf<XOR8>(x);
  x = fold16(x, x); return fold32(x, x);
}
__device__ __forceinline__ float wave_max(float x) {
  x = fmaxf(x, dppf<XOR1>(x)); x = fmaxf(x, dppf<XOR2>(x)); x = fmaxf(x, dppf<XOR7>(x)); x = fmaxf(x, dppf<XOR8>(x));
  auto s = __builtin_amdgcn_permlane16_swap(__float_as_uint(x), __float_as_uint(x), false, false);
  x = fmaxf(__uint_as_float(s[0]), __uint_as_float(s[1]));
  auto t = __builtin_amdgcn_permlane32_swap(__float_as_uint(x), __float_as_uint(x), false, false);
  return fmaxf(__uint_as_float(t[0]), __uint_as_float(t[1]));
}
template <int OFF> __device__ __forceinline__ void ld_nt(u32x4& d, u32 voff, const char* sbase) {
  asm volatile("global_load_dwordx4 %0, %1, %2 offset:%3 nt" : "=v"(d) : "v"(voff), "s"(sbase), "i"(OFF));
}
template <int OFF> __device__ __forceinline__ void ld_pl(u32x4& d, u32 voff, const char* sbase) {
  asm volatile("global_load_dwordx4 %0, %1, %2 offset:%3" : "=v"(d) : "v"(voff), "s"(sbase), "i"(OFF));
}
template <int N> __device__ __forceinline__ void waitv(u32x4& d) { asm volatile("s_waitcnt vmcnt(%1)" : "+v"(d) : "i"(N)); }
__device__ __forceinline__ void pin8(float (&a)[8]) {
  asm volatile("" : "+v"(a[0]), "+v"(a[1]), "+v"(a[2]), "+v"(a[3]), "+v"(a[4]), "+v"(a[5]), "+v"(a[6]), "+v"(a[7]));
}
__device__ __forceinline__ void axpy8(float (&acc)[8], const u32x4& w, u32 slo, u32 shi) {
#pragma unroll
  for (int k = 0; k < 4; ++k) { acc[2 * k] = dot2(w[k], slo, acc[2 * k]); acc[2 * k + 1] = dot2(w[k], shi, acc[2 * k + 1]); }
}
__device__ __forceinline__ u32 r24(float v) { return __float_as_uint(v) + 0x80u; }
__device__ __forceinline__ u64 gran(u32 a, u32 b, u32 tag) {
  return (u64)((a & 0xffffff00u) | (tag & 0xffu)) | ((u64)((b & 0xffffff00u) | ((tag >> 8) & 0xffu)) << 32);
}
__device__ __forceinline__ u32 gtag(u64 g) { return __builtin_amdgcn_perm((u32)(g >> 32), (u32)g, 0x0c0c0400u); }
__device__ __forceinline__ float ga(u64 g) { return __uint_as_float((u32)g & 0xffffff00u); }
__device__ __forceinline__ float gb(u64 g) { return __uint_as_float((u32)(g >> 32) & 0xffffff00u); }
__device__ __forceinline__ void put(gu64* p, u64 g) { __hip_atomic_store(p, g, RLX_AGENT); }
__device__ __forceinline__ u64 get(gu64* p) { return __hip_atomic_load(p, RLX_AGENT); }
__device__ __forceinline__ void get_async(u32x2& d, gu64* p) { asm volatile("global_load_dwordx2 %0, %1, off sc1" : "=v"(d) : "v"(p)); }
__device__ __forceinline__ u32 tag2(const u32x2& g) { return __builtin_amdgcn_perm(g[1], g[0], 0x0c0c0400u); }
template <int NAP = 1> __device__ __forceinline__ bool lds_wait(LAS u32* w, u32 want) {
#pragma nounroll
  for (u32 spins = 0; spins < (SPIN_CAP << 4); ++spins) {
    if ((u32)__builtin_amdgcn_readfirstlane(__hip_atomic_load(w, RLX_WG)) >= want) { asm volatile("" ::: "memory"); return true; }
    __builtin_amdgcn_s_sleep(NAP);
  }
  return false;
}

template <int N> __device__ __forceinline__ bool poll(gu64* const (&p)[N], const u32 (&e)[N], u64 (&v)[N]) {
#pragma nounroll
  for (u32 spins = 0; spins < SPIN_CAP; ++spins) {
    u32 d = 0;
#pragma unroll
    for (int k = 0; k < N; ++k) v[k] = get(p[k]);
#pragma unroll
    for (int k = 0; k < N; ++k) d |= gtag(v[k]) ^ e[k];
    if (__all(d == 0)) return true;
  }
  return false;
}

struct KV { const char* k; const char* v; u32 pos, stride, wrap; int nt; };
constexpr int NBUF = 64 + DR;
template <int N> __device__ __forceinline__ void kv_issue(u32x4& d, const KV& a, int tile, u32 voff) {
  const int blk = tile * 4 + (N >> 2);
  const bool isv = blk >= a.nt * 4;
  u32 off = a.pos + (u32)(isv ? blk - a.nt * 4 : blk) * a.stride;
  off = off >= a.wrap ? off - a.wrap : off;
  ld_nt<(N & 3) * 1024>(d, voff, (isv ? a.v : a.k) + off);
}
template <int A, int B> __device__ __forceinline__ void kv_issue_range(u32x4 (&buf)[NBUF], const KV& a, int tile, u32 voff) {
  if constexpr (A < B) { kv_issue<A>(buf[A], a, tile, voff); kv_issue_range<A + 1, B>(buf, a, tile, voff); }
}
template <int LEN_, int I0_, int I1_, int DD_, int THR_> struct Sched {
  static constexpr int LEN = LEN_, I0 = I0_, I1 = I1_, DD = DD_, THR = THR_;
  static constexpr int pos(int n) { return (I0 > DD && 5 * (n - I0) + 4 < n - DD) ? 5 * (n - I0) + 4 : n - DD; }
  static constexpr int issued(int step) { int n = I0; while (step > 0 && n < LEN + I1 && pos(n) < step) ++n; return n; }
  static_assert(issued(LEN) == LEN + I1 && LEN + I1 <= NBUF, "schedule");
};
template <class S, int N>
__device__ __forceinline__ void kv_step(u32x4 (&buf)[NBUF], const KV& a, int tile, u32 voff, u32 thr) {
  constexpr int W = S::issued(N) - 1 - N;
  if constexpr (S::THR < 0)
    asm volatile("s_waitcnt vmcnt(%2)\n\ts_cmp_lt_u32 %1, 1\n\ts_cbranch_scc1 1f\n\ts_waitcnt vmcnt(%3)\n\ts_cmp_lt_u32 %1, 2\n\ts_cbranch_scc1 1f\n\ts_waitcnt vmcnt(%4)\n1:"
                 : "+v"(buf[N]) : "s"(thr), "i"(W), "i"(W - 1), "i"(W - 2) : "scc");
  else waitv<W - S::THR>(buf[N]);
  kv_issue_range<S::issued(N), S::issued(N + 1)>(buf, a, tile, voff);
}
template <class S, int T, int END>
__device__ __forceinline__ void k_steps(u32x4 (&buf)[NBUF], const KV& a, int tile, u32 voff, u32 thr, const u32x4& q, float (&x)[16]) {
  if constexpr (T < END) {
    kv_step<S, T>(buf, a, tile, voff, thr);
    float d = 0.f;
#pragma unroll
    for (int k = 0; k < 4; ++k) d = dot2(buf[T][k], q[k], d);
    asm volatile("" : "+v"(d));
    x[T & 15] = d;
    k_steps<S, T + 1, END>(buf, a, tile, voff, thr, q, x);
  }
}
template <class S, int G = 0>
__device__ __forceinline__ void k_run(u32x4 (&buf)[NBUF], const KV& a, int tile, u32 voff, u32 thr, int c, const u32x4& q, LAS float* sc, float& mx) {
  if constexpr (G < S::LEN / 16) {
    float x[16];
    k_steps<S, G * 16, G * 16 + 16>(buf, a, tile, voff, thr, q, x);
#define TR_STEP(HALF, CTRL, BIT) _Pragma("unroll") for (int i = 0; i < (HALF); ++i) { \
      const float lo_ = x[i], hi_ = x[i + (HALF)]; const float keep = (c & (BIT)) ? hi_ : lo_, send = (c & (BIT)) ? lo_ : hi_; x[i] = keep + dppf<CTRL>(send); }
    TR_STEP(8, XOR8, 8) TR_STEP(4, XOR7, 4) TR_STEP(2, XOR2, 2) TR_STEP(1, XOR1, 1)
#undef TR_STEP
    const float s2 = rbf(rbf(x[0]) * 0.08838834764831845f) * 1.4426950408889634f;
    sc[(tile + G) * 64] = s2;
    mx = fmaxf(mx, s2);
    asm volatile("" : "+v"(mx));
    k_run<S, G + 1>(buf, a, tile, voff, thr, c, q, sc, mx);
  } else {
#pragma unroll
    for (int i = 0; i < S::I1; ++i) buf[i] = buf[S::LEN + i];
  }
}
template <class S, int T>
__device__ __forceinline__ void v_steps(u32x4 (&buf)[NBUF], const KV& a, int tile, u32 voff, u32 thr, u32 plo, u32 phi, float (&o)[8]) {
  if constexpr (T < 16) {
    kv_step<S, T>(buf, a, tile, voff, thr);
    axpy8(o, buf[T], dppu<BCAST + T>(plo), dppu<BCAST + T>(phi));
    pin8(o);
    v_steps<S, T + 1>(buf, a, tile, voff, thr, plo, phi, o);
  }
}
template <class S>
__device__ __forceinline__ void v_run(u32x4 (&buf)[NBUF], const KV& a, int tile, u32 voff, u32 thr, float p, float (&o)[8]) {
  const u32 plo = cvtpk(p, 0.f), phi = plo << 16;
  v_steps<S, 0>(buf, a, tile, voff, thr, plo, phi, o);
#pragma unroll
  for (int i = 0; i < S::I1; ++i) buf[i] = buf[16 + i];
}
__device__ __forceinline__ float max24(float mx) { return __uint_as_float(r24(wave_max(mx)) & 0xffffff00u); }
template <int N, int END, int DD> __device__ __forceinline__ void kv_pre(u32x4 (&buf)[NBUF], const KV& a, int tile, u32 voff) {
  if constexpr (N < END) {
    if constexpr (N >= DD) asm volatile("s_waitcnt vmcnt(%0)" :: "i"(DD - 1));
    kv_issue<N>(buf[N], a, tile, voff); kv_pre<N + 1, END, DD>(buf, a, tile, voff);
  }
}
__device__ __forceinline__ f32x2 fold8(const float (&o)[8]) {
  return f32x2{fold16(fold32(o[0], o[4]), fold32(o[2], o[6])), fold16(fold32(o[1], o[5]), fold32(o[3], o[7]))};
}
__device__ __forceinline__ float merge4(LAS char* lds, int lane, float& M, float& L) {
  const f32x2 v = *(LAS f32x2*)(lds + L_AM + (lane & 3) * 8);
  M = fmaxf(v[0], dppf<XOR1>(v[0])); M = fmaxf(M, dppf<XOR2>(M));
  const float e = __builtin_amdgcn_exp2f(v[0] - M);
  L = e * v[1]; L += dppf<XOR1>(L); L += dppf<XOR2>(L);
  return e;
}

template <int I> __device__ __forceinline__ void q_issue(u32x4 (&w)[18], u32 voff, const char* base) {
  if constexpr (I < 18) { ld_nt<0>(w[I], voff, base + (size_t)I * (H * DH * 2)); q_issue<I + 1>(w, voff, base); }
}
template <int I, int YOUNGER> __device__ __forceinline__ void q_steps(u32x4 (&w)[18], const u32 (&xp)[9], float (&acc)[8]) {
  if constexpr (I < 18) {
    waitv<YOUNGER + 17 - I>(w[I]);
    const u32 p = xp[I >> 1];
    axpy8(acc, w[I], (I & 1) ? p >> 16 : p & 0xffffu, (I & 1) ? p & 0xffff0000u : p << 16);
    pin8(acc);
    q_steps<I + 1, YOUNGER>(w, xp, acc);
  }
}

struct FF { const char* g; const char* u; const char* d; u32 vo, v1, v2; };
template <int N> __device__ __forceinline__ void gu_ld(u32x4& w, const FF& f, int it) {
  ld_nt<0>(w, f.vo, ((N & 1) ? f.u : f.g) + (size_t)(it * 8 + (N >> 1)) * (F * 2));
}
template <int N> __device__ __forceinline__ void dn_ld(u32x4& w, const FF& f, int it) {
  constexpr int e = N % 9;
  const char* base = f.d + (size_t)((it + N / 9) * 8) * (D * 2);
  if constexpr (e < 8) ld_nt<0>(w, f.v1, base + e * (D * 2)); else ld_nt<0>(w, f.v2, base);
}
template <int FD, bool LAST, int N>
__device__ __forceinline__ void gu_round(u32x4 (&w)[32], const FF& f, int it, const u32x4 xp, float (&g)[8], float (&u)[8]) {
  if constexpr (N < 16) {
    waitv<FD - 1>(w[N]);
    if constexpr (LAST && N + FD >= 16) dn_ld<N + FD - 16>(w[N + FD], f, 0); else gu_ld<N + FD>(w[N + FD], f, it);
    const u32 p = xp[N >> 2], lo = (N & 2) ? p >> 16 : p & 0xffffu, hi = (N & 2) ? p & 0xffff0000u : p << 16;
    if constexpr (N & 1) { axpy8(u, w[N], lo, hi); pin8(u); } else { axpy8(g, w[N], lo, hi); pin8(g); }
    gu_round<FD, LAST, N + 1>(w, f, it, xp, g, u);
  } else {
#pragma unroll
    for (int i = 0; i < FD; ++i) w[i] = w[16 + i];
  }
}
template <int FD, bool LAST, int N>
__device__ __forceinline__ void dn_round(u32x4 (&w)[32], const FF& f, int it, u32 hv, u32 hr0, u32 hr1, float (&ym)[8], float (&yr)[8]) {
  if constexpr (N < 18) {
    constexpr bool more = !LAST || N + FD < 18;
    waitv<(more ? FD - 1 : 17 - N)>(w[N]);
    if constexpr (more) dn_ld<N + FD>(w[N + FD], f, it);
    if constexpr (N % 9 < 8) { const u32 lo = __builtin_amdgcn_readlane(hv, (it + N / 9) * 8 + N % 9); axpy8(ym, w[N], lo, lo << 16); pin8(ym); }
    else { const u32 hr = N < 9 ? hr0 : hr1; axpy8(yr, w[N], hr, hr << 16); pin8(yr); }
    dn_round<FD, LAST, N + 1>(w, f, it, hv, hr0, hr1, ym, yr);
  } else if constexpr (!LAST) {
#pragma unroll
    for (int i = 0; i < FD; ++i) w[i] = w[18 + i];
  }
}
template <int N, int END> __device__ __forceinline__ void gu_pre(u32x4 (&w)[32], const FF& f) {
  if constexpr (N < END) { gu_ld<N>(w[N], f, 0); gu_pre<N + 1, END>(w, f); }
}

__global__ void __launch_bounds__(512, 2) fused_decode(const char* __restrict__ x, const char* __restrict__ kc, const char* __restrict__ vc,
    const char* __restrict__ wq, const char* __restrict__ wo, const char* __restrict__ wg, const char* __restrict__ wu,
    const char* __restrict__ wd, u16* __restrict__ out, char* ws) {
  __shared__ __attribute__((aligned(16))) char smem[L_END];
  LAS char* lds = (LAS char*)smem;
  LAS u32* flags = (LAS u32*)(lds + L_FL);
  const int lane = threadIdx.x & 63, wv = __builtin_amdgcn_readfirstlane(threadIdx.x >> 6);
  const int b = blockIdx.x, j = b >> 3;
  const int r = lane >> 4, c = lane & 15;
  const u64 tstart = __builtin_amdgcn_s_memrealtime();

  if (__builtin_expect(j >= 4, 1)) {
    if (__builtin_expect(wv > 4, 0)) __builtin_amdgcn_endpgm();
    const int hq = ((j - 4) * 37) >> 8, s = j - 4 - hq * NATT, h = (b & 7) * 4 + hq;
    if (__builtin_expect(wv == 4, 0)) {
      gu64* const qs = (gu64*)(ws + WS_Q) + h * 64 + lane;
      gu64* const ml = (gu64*)(ws + WS_ML) + (h * 8 + s) * 8;
      const u32 tq = gtag(get(qs));
      bool got = false; u64 qg = 0;
#pragma nounroll
      while ((u32)(__builtin_amdgcn_s_memrealtime() - tstart) < Q_DUE) __builtin_amdgcn_s_sleep(4);
#pragma nounroll
      for (u32 spins = 0; spins < SPIN_CAP; ++spins) {
        const u64 v = get(qs);
        if (gtag(v) != tq) { got = true; qg = v; }
        if (__all(got)) break;
        __builtin_amdgcn_s_sleep(4);
      }
      const u32 lo = (u32)qg, hi = (u32)(qg >> 32), tag = ((lo >> 8) & 0xffu) | (hi & 0xff00u);
      ((LAS u32*)(lds + L_Q))[lane] = (lo >> 16) | (hi & 0xffff0000u);
      got = __all(got);
      if (lane < 5) flags[lane] = lane ? 0u : got ? tag | 0x10000u : 0u;
      asm volatile("s_waitcnt lgkmcnt(0)" ::: "memory");
      if (got && lane == 0) put(ml + 2, gran(0, 0, tag));
      __builtin_amdgcn_s_barrier();
      if (lds_wait<8>(flags + 3, 4u)) {
        float M, L;
        const float e = merge4(lds, lane, M, L);
        if (lane < 4) *(LAS float*)(lds + L_AM + 32 + lane * 4) = e;
        asm volatile("s_waitcnt lgkmcnt(0)" ::: "memory");
        if (lane == 0) { __hip_atomic_store(flags + 4, 1u, RLX_WG); if (got) put(ml, gran(__float_as_uint(M), r24(L), tag)); }
      }
      if (lds_wait<8>(flags + 2, 1u) && got && lane == 0) put(ml + 1, gran(0, 0, tag));
      __builtin_amdgcn_endpgm();
    }
    const KV a{kc + ((size_t)h << 22), vc + ((size_t)h << 22), (u32)(h * 28 + s * 4 + wv) * 4096u, 28u * 4096u, 36u * 28u * 4096u, 9};
    const u32 voff = lane * 16;
    u32x4 buf[NBUF];
    kv_pre<0, PRE, D1>(buf, a, 0, voff);
    __builtin_amdgcn_s_barrier();
    asm volatile("" ::: "memory");
    const u32x4 q = *(LAS u32x4*)(lds + L_Q + c * 16);
    const u32 tag = __builtin_amdgcn_readfirstlane(flags[0]);
    u64 tg = gran(0, 0, tag);
    asm volatile("" : "+s"(ws));
    gu64* po = (gu64*)(ws + WS_PO) + (h * 8 + s) * 64 + lane;
    asm volatile("" : "+v"(tg), "+v"(po));
    auto pace = [&](int t) -> u32 {
      const u32 now = (u32)(__builtin_amdgcn_s_memrealtime() - tstart);
      const u32 due = t < T2 ? PACE_T4 + (t - 4) * PACE_1 : PACE_T4 + (T2 - 4) * PACE_1 + (t - T2) * PACE_2;
      return __builtin_amdgcn_readfirstlane(DR - (t < T2 ? D1 : D2) - (now > due));
    };
    LAS float* sc = (LAS float*)(lds + L_WO) + wv * (9 * 64) + lane;
    float mx = -INFINITY;
    k_run<Sched<64, PRE, DR, DR, DR - D1>>(buf, a, 0, voff, 0, c, q, sc, mx);
    for (int t = 4; t < 9; ++t) k_run<Sched<16, DR, DR, DR, -1>>(buf, a, t, voff, pace(t), c, q, sc, mx);
    float o[8] = {}, sn = sc[64];
    const float m = max24(mx);
    float p = __builtin_amdgcn_exp2f(sc[0] - m), l = p;
    for (int t = 9; t < 17; ++t) {
      const float pn = __builtin_amdgcn_exp2f(sn - m);
      l += pn;
      if (t < 16) sn = sc[(t - 7) * 64];
      else {
        const float ls = wave_sum(l);
        if (lane == 0) *(LAS f32x2*)(lds + L_AM + wv * 8) = f32x2{m, ls};
        asm volatile("s_waitcnt lgkmcnt(0)" ::: "memory");
        if (lane == 0) __hip_atomic_fetch_add(flags + 3, 1u, RLX_WG);
      }
      v_run<Sched<16, DR, DR, DR, -1>>(buf, a, t, voff, pace(t), p, o);
      p = pn;
    }
    if (wv == 0) __hip_atomic_store(flags + 2, 1u, RLX_WG);
    const bool ok = lds_wait(flags + 4, 1u) && tag != 0;
    float cw = *(LAS float*)(lds + L_AM + 32 + wv * 4);
    asm volatile("" : "+v"(cw));
    v_run<Sched<16, DR, 0, DL, 0>>(buf, a, 17, voff, 0, p, o);
    *(LAS f32x2*)(lds + L_AO + wv * 512 + lane * 8) = cw * fold8(o);
    asm volatile("s_waitcnt lgkmcnt(0)" ::: "memory");
    u32 old = 0;
    if (lane == 0) old = __hip_atomic_fetch_add(flags + 1, 1u, RLX_WG);
    if (__builtin_amdgcn_readfirstlane(old) == 3 && ok) {
      asm volatile("" ::: "memory");
      LAS f32x2* ao = (LAS f32x2*)(lds + L_AO) + lane;
      const f32x2 y = (ao[0] + ao[64]) + (ao[128] + ao[192]);
      put(po, tg | gran(r24(y[0]), r24(y[1]), 0));
    }
    __builtin_amdgcn_endpgm();
  }

  const int h = (b & 7) * 4 + j, unit0 = (b & 7) * 9 + j * 2, nunit = j == 3 ? 3 : 2;
  gu64* const ML = (gu64*)(ws + WS_ML);
  gu64* const PO = (gu64*)(ws + WS_PO);
  gu64* const YA = (gu64*)(ws + WS_YA);
  gu64* const qs = (gu64*)(ws + WS_Q) + h * 64 + lane;
  u32 tq = 0;
  {
    u32x4 xa, xb, w[18];
    ld_pl<0>(xa, lane * 16, x);
    ld_pl<1024>(xb, (lane & 7) * 16, x);
    q_issue<0>(w, (u32)(r * 18) * (H * DH * 2) + c * 16, wq + (size_t)(wv * 72) * (H * DH * 2) + h * (DH * 2));
    u32x2 fa, fq;
    get_async(fa, (wv < NATT ? PO + (h * 8 + wv) * 64 : ML + h * 64) + lane);
    get_async(fq, qs);
    if (wv == 7 && lane < 5) flags[lane] = 0;
    asm volatile("s_waitcnt vmcnt(20)" : "+v"(xa), "+v"(xb));
    float xv[16], sum = 0.f;
    const bool ext = lane < 8;
#pragma unroll
    for (int k = 0; k < 4; ++k) { xv[2 * k] = lo2f(xa[k]); xv[2 * k + 1] = hi2f(xa[k]); xv[8 + 2 * k] = lo2f(xb[k]); xv[9 + 2 * k] = hi2f(xb[k]); }
    float se = 0.f;
#pragma unroll
    for (int k = 0; k < 8; ++k) { sum += xv[k]; se += xv[8 + k]; }
    const float mean = wave_sum(ext ? sum + se : sum) * (1.f / D);
    float ss = 0.f; se = 0.f;
#pragma unroll
    for (int k = 0; k < 8; ++k) { const float d0 = xv[k] - mean, d1 = xv[8 + k] - mean; ss = fmaf(d0, d0, ss); se = fmaf(d1, d1, se); }
    const float var = wave_sum(ext ? ss + se : ss) * (1.f / D);
    const float mb = rbf(mean), inv = __builtin_amdgcn_rcpf(rbf(__builtin_amdgcn_sqrtf(rbf(rbf(var) + 1e-5f))));
    u32x4 na, nb;
#pragma unroll
    for (int k = 0; k < 4; ++k) {
      na[k] = cvtpk(rbf(xv[2 * k] - mb) * inv, rbf(xv[2 * k + 1] - mb) * inv);
      nb[k] = cvtpk(rbf(xv[8 + 2 * k] - mb) * inv, rbf(xv[9 + 2 * k] - mb) * inv);
    }
    *(LAS u32x4*)(lds + L_XN + lane * 16) = na;
    if (ext) *(LAS u32x4*)(lds + L_XN + 1024 + lane * 16) = nb;
    asm volatile("s_waitcnt lgkmcnt(0)" ::: "memory");
    u32 xp[9];
#pragma unroll
    for (int k = 0; k < 9; ++k) xp[k] = *(LAS u32*)(lds + L_XN + wv * 144 + r * 36 + k * 4);
    float acc[8];
#pragma unroll
    for (int k = 0; k < 8; ++k) acc[k] = 0.f;
    q_steps<0, 2>(w, xp, acc);
    *(LAS f32x2*)(lds + L_Q + wv * 512 + c * 32 + r * 8) = fold8(acc);
    asm volatile("s_waitcnt vmcnt(0)" : "+v"(fa), "+v"(fq));
    ((LAS u32*)(lds + L_AO))[wv * 64 + lane] = tag2(fa);
    tq = tag2(fq);
    asm volatile("s_waitcnt lgkmcnt(0)" ::: "memory");
    __builtin_amdgcn_s_barrier();
    asm volatile("" ::: "memory");
  }
  u32x4 q;
  if (wv == 7 || wv < 4) {
    const int o = wv == 7 ? (lane >> 2) * 32 : c * 32;
    f32x4 q0 = *(LAS f32x4*)(lds + L_Q + o), q1 = *(LAS f32x4*)(lds + L_Q + o + 16);
#pragma unroll
    for (int i = 1; i < 8; ++i) { q0 += *(LAS f32x4*)(lds + L_Q + i * 512 + o); q1 += *(LAS f32x4*)(lds + L_Q + i * 512 + o + 16); }
    q = u32x4{cvtpk(q0[0], q0[1]), cvtpk(q0[2], q0[3]), cvtpk(q1[0], q1[1]), cvtpk(q1[2], q1[3])};
    asm volatile("" : "+v"(q));
  }
  u32 qa = 0, qb = 0;
  if (wv == 7) {
    u32 t[8];
#pragma unroll
    for (int k = 0; k < 8; ++k) t[k] = ((LAS u32*)(lds + L_AO))[k * 64 + lane];
    u32 e = __builtin_amdgcn_readfirstlane(t[7]);
    bool used;
#pragma nounroll
    do {
      e = (e + 1) & 0xffffu;
      used = false;
#pragma unroll
      for (int k = 0; k < 8; ++k) used |= t[k] == e;
    } while (__any(used));
    const u32 qv = (lane & 2) ? ((lane & 1) ? q[3] : q[2]) : ((lane & 1) ? q[1] : q[0]);
    qa = (qv << 16) | ((e & 0xffu) << 8); qb = (qv & 0xffff0000u) | (e & 0xff00u);
    put(qs, gran(qa, qb, ++tq));
    if (lane == 0) flags[5] = e;
    asm volatile("s_waitcnt lgkmcnt(0)" ::: "memory");
  }
  __builtin_amdgcn_s_barrier();
  asm volatile("" ::: "memory");
  const u32 tag = __builtin_amdgcn_readfirstlane(flags[5]);

  if (wv == 7) {
    gu32* const go = (gu32*)(ws + WS_GO);
    gu32* const hb = (gu32*)(ws + WS_HB) + (b == 0 ? lane & 31 : b) * 16;
    gu64* const ak = ML + (h * 8 + (lane & 7)) * 8 + 2;
    u32 g0 = __hip_atomic_load(go, RLX_AGENT), h0 = __hip_atomic_load(hb, RLX_AGENT);
    asm volatile("s_waitcnt vmcnt(0)" : "+v"(g0), "+v"(h0) :: "memory");
    bool ok = false;
#pragma nounroll
    for (u32 spins = 0; spins < SPIN_CAP; ++spins) {
      if (__all((lane & 7) == 7 || gtag(get(ak)) == tag)) { ok = true; break; }
      if ((spins & 3) == 3) put(qs, gran(qa, qb, ++tq));
      __builtin_amdgcn_s_sleep(8);
    }
    if (ok && lds_wait(flags + 4, nunit)) {
      ok = false;
      bool seen = (lane & 31) == 0;
#pragma nounroll
      for (u32 spins = 0; spins < SPIN_CAP; ++spins) {
        if (b == 0) {
          seen |= __hip_atomic_load(hb, RLX_AGENT) != h0;
          if (__all(seen)) { if (lane == 0) __hip_atomic_store(go, g0 + 1, RLX_AGENT); ok = true; break; }
        } else {
          if (lane == 0) __hip_atomic_store(hb, ++h0, RLX_AGENT);
          if (__all(__hip_atomic_load(go, RLX_AGENT) != g0)) { ok = true; break; }
        }
        __builtin_amdgcn_s_sleep(8);
      }
      if (ok && lane == 0) __hip_atomic_store(flags, 1u, RLX_WG);
    }
    return;
  }
  if (wv >= 4) {
    if (wv - 4 >= nunit) return;
    const int unit = unit0 + wv - 4;
    LAS u32* hbuf = (LAS u32*)(lds + L_HB) + (wv - 4) * 64;
    const int rr = lane >> 3, cc = lane & 7;
    const FF f{wg + unit * 128, wu + unit * 128, wd + (size_t)(unit * 64) * (D * 2),
               (u32)(rr * 72) * (F * 2) + cc * 16, (u32)lane * 16, (u32)rr * (D * 2) + 1024 + cc * 16};
    float g[8], u[8];
#pragma unroll
    for (int k = 0; k < 8; ++k) { g[k] = 0.f; u[k] = 0.f; }
    u32x4 w[32];
    gu_pre<0, FD>(w, f);
    for (int it = 0; it < 8; ++it) gu_round<FD, false, 0>(w, f, it, *(LAS u32x4*)(lds + L_XN + rr * 144 + it * 16), g, u);
    gu_round<FD, true, 0>(w, f, 8, *(LAS u32x4*)(lds + L_XN + rr * 144 + 128), g, u);
    const bool b3 = lane & 8;
    const int col = 2 * r + (b3 ? 1 : 0);
    const f32x2 g2 = fold8(g), u2 = fold8(u);
    const float gs = (b3 ? g2[1] : g2[0]) + dppf<XOR8>(b3 ? g2[0] : g2[1]), us = (b3 ? u2[1] : u2[0]) + dppf<XOR8>(b3 ? u2[0] : u2[1]);
    hbuf[cc * 8 + col] = cvtpk(rbf(gs) * rbf(us), 0.f);
    asm volatile("s_waitcnt lgkmcnt(0)" ::: "memory");
    const u32 hv = hbuf[lane];
    float ym[8], yr[8];
#pragma unroll
    for (int k = 0; k < 8; ++k) { ym[k] = 0.f; yr[k] = 0.f; }
    for (int it = 0; it < 6; it += 2) dn_round<FD, false, 0>(w, f, it, hv, hbuf[it * 8 + rr], hbuf[it * 8 + 8 + rr], ym, yr);
    dn_round<FD, true, 0>(w, f, 6, hv, hbuf[48 + rr], hbuf[56 + rr], ym, yr);
    const f32x2 y2 = fold8(yr);
    LAS float* yf = (LAS float*)(lds + L_YF) + (wv - 4) * D;
    *(LAS f32x4*)(yf + lane * 8) = f32x4{ym[0], ym[1], ym[2], ym[3]};
    *(LAS f32x4*)(yf + lane * 8 + 4) = f32x4{ym[4], ym[5], ym[6], ym[7]};
    yf[512 + cc * 8 + col] = (b3 ? y2[1] : y2[0]) + dppf<XOR8>(b3 ? y2[0] : y2[1]);
    asm volatile("s_waitcnt lgkmcnt(0)" ::: "memory");
    if (lane == 0) __hip_atomic_fetch_add(flags + 3, 1u, RLX_WG);
    return;
  }
  gu64* const yi = YA + (unit0 + (wv < nunit ? wv : 0)) * (H * 8) + lane;
  u32 ey[4];
  {
    const u32 voff = lane * 16;
    const KV a{kc + ((size_t)h << 22), vc + ((size_t)h << 22), (u32)(1008 + wv * 4) * 4096u, 4096u, 1u << 30, 1};
    u32x4 buf[NBUF];
    float mx = -INFINITY;
    {
      u32x2 fy[4];
#pragma unroll
      for (int k = 0; k < 4; ++k) get_async(fy[k], yi + k * 64);
      kv_pre<0, SD, SD>(buf, a, 0, voff);
      k_run<Sched<16, SD, 0, SD, 0>>(buf, a, 0, voff, 0, c, q, (LAS float*)(lds + L_AO + wv * 512) + lane, mx);
      asm volatile("" : "+v"(fy[0]), "+v"(fy[1]), "+v"(fy[2]), "+v"(fy[3]));
#pragma unroll
      for (int k = 0; k < 4; ++k) ey[k] = (tag2(fy[k]) + 1) & 0xffffu;
      if (wv < nunit && lane == 0) __hip_atomic_fetch_add(flags + 4, 1u, RLX_WG);
    }
    const char* wb = wo + (size_t)(h * DH + wv * 32) * (D * 2);
#pragma nounroll
    for (int it = 0; it < 36 / SD; ++it) {
      u32x4 t[SD];
#pragma unroll
      for (int k = 0; k < SD; ++k) ld_nt<0>(t[k], voff, wb + (it * SD + k) * 1024);
#pragma unroll
      for (int k = 0; k < SD; ++k) { waitv<0>(t[k]); *(LAS u32x4*)(lds + L_WO + wv * (32 * D * 2) + (it * SD + k) * 1024 + lane * 16) = t[k]; }
    }
    kv_pre<0, SD, SD>(buf, a, 1, voff);
    const float m = max24(mx), p = __builtin_amdgcn_exp2f(mx - m);
    const f32x2 ms = {m, wave_sum(p)};
    float o[8] = {};
    v_run<Sched<16, SD, 0, SD, 0>>(buf, a, 1, voff, 0, p, o);
    *(LAS f32x2*)(lds + L_AO + wv * 512 + lane * 8) = fold8(o);
    if (lane == 0) *(LAS f32x2*)(lds + L_AM + wv * 8) = ms;
    asm volatile("s_waitcnt lgkmcnt(0)" ::: "memory");
    if (lane == 0) __hip_atomic_fetch_add(flags + 2, 1u, RLX_WG);
  }
  gu64* ya[3]; u32 ty[3];
#pragma unroll
  for (int i = 0; i < 3; ++i) {
    const int col = wv * 144 + i * 64 + (i < 2 ? lane : c);
    ya[i] = YA + ((col >> 3) * H + h) * 8 + (col & 7);
    ty[i] = gtag(get(ya[i])) + 1;
  }
  float xres = lo2f(((const u16*)x)[(unit0 + (wv < nunit ? wv : 0)) * 8 + (lane & 7)]);
  GAS u16* op = (GAS u16*)out + (unit0 + wv) * 8 + (lane & 7);
  gu64* const ml = ML + (h * 8 + (lane & 7)) * 8;
  gu64* const po = PO + h * 512 + lane;
  bool ok = lds_wait(flags + 2, 4u);
  u32x4 Bf[9][4];
#pragma unroll
  for (int t = 0; t < 9; ++t)
#pragma unroll
    for (int k = 0; k < 4; ++k) {
#pragma unroll
      for (int i = 0; i < 4; ++i) {
        LAS u16* p = (LAS u16*)(lds + L_WO + (32 * k + 8 * i + 2 * r) * (D * 2) + (wv * 144 + 16 * t + c) * 2);
        Bf[t][k][i] = (u32)p[0] | ((u32)p[D] << 16);
      }
      asm volatile("" : "+v"(Bf[t][k]));
    }
  ok &= lds_wait(flags + 3, nunit);
  u64 fg[3];
#pragma unroll
  for (int i = 0; i < 3; ++i) {
    LAS float* yf = (LAS float*)(lds + L_YF) + wv * 144 + i * 64 + (i < 2 ? lane : c);
    float y = yf[0] + yf[D];
    if (j == 3) y += yf[2 * D];
    fg[i] = gran(0, r24(y), ty[i]);
  }
  float wgt;
  f32x2 o;
  {
    float M, L;
    const float e4 = merge4(lds, lane, M, L);
    LAS f32x2* ao = (LAS f32x2*)(lds + L_AO) + lane;
    o = (dppf<0x00>(e4) * ao[0] + dppf<0x55>(e4) * ao[64]) + (dppf<0xAA>(e4) * ao[128] + dppf<0xFF>(e4) * ao[192]);
    const bool own = (lane & 7) == 7;
    u64 v = 0;
    bool in = false;
#pragma nounroll
    for (u32 spins = 0; spins < SPIN_CAP && !in; ++spins) {
      v = get(ml);
      in = __all(own || gtag(v) == tag);
      if (!in) __builtin_amdgcn_s_sleep(16);
    }
    ok &= in;
    const float m = own ? M : ga(v), l = own ? L : gb(v);
    M = fmaxf(m, dppf<XOR1>(m)); M = fmaxf(M, dppf<XOR2>(M)); M = fmaxf(M, dppf<XOR7>(M));
    const float e = __builtin_amdgcn_exp2f(m - M);
    L = e * l; L += dppf<XOR1>(L); L += dppf<XOR2>(L); L += dppf<XOR7>(L);
    wgt = e * __builtin_amdgcn_rcpf(L);
    o *= dppf<BCAST + 7>(wgt);
  }
  float w7[7] = {dppf<BCAST + 0>(wgt), dppf<BCAST + 1>(wgt), dppf<BCAST + 2>(wgt), dppf<BCAST + 3>(wgt), dppf<BCAST + 4>(wgt), dppf<BCAST + 5>(wgt), dppf<BCAST + 6>(wgt)};
  asm volatile("" : "+v"(fg[0]), "+v"(fg[1]), "+v"(fg[2]), "+v"(ya[0]), "+v"(ya[1]), "+v"(ya[2]), "+v"(xres), "+v"(op), "+v"(o));
  asm volatile("" : "+v"(w7[0]), "+v"(w7[1]), "+v"(w7[2]), "+v"(w7[3]), "+v"(w7[4]), "+v"(w7[5]), "+v"(w7[6]));
  ok &= lds_wait(flags, 1u);
  if (!ok) return;
#pragma nounroll
  for (u32 spins = 0; spins < SPIN_CAP; ++spins) {
    if (__all((lane & 7) == 7 || gtag(get(ml + 1)) == tag)) break;
    __builtin_amdgcn_s_sleep(4);
  }
  u32x2 pv[7] = {};
  u32 have = 0;
#pragma nounroll
  for (u32 spins = 0;; ++spins) {
    if (spins == SPIN_CAP) return;
#define GET(k) asm volatile("s_bitcmp1_b32 %2, " #k "\n\ts_cbranch_scc1 1f\n\tglobal_load_dwordx2 %0, %1, off offset:%3 sc1\n1:" : "+v"(pv[k]) : "v"(po), "s"(have), "i"(k * 512) : "scc", "memory");
    GET(0) GET(1) GET(2) GET(3) GET(4) GET(5) GET(6)
#undef GET
    asm volatile("s_waitcnt vmcnt(0)" : "+v"(pv[0]), "+v"(pv[1]), "+v"(pv[2]), "+v"(pv[3]), "+v"(pv[4]), "+v"(pv[5]), "+v"(pv[6]));
    u32 d[7];
#pragma unroll
    for (int k = 0; k < 7; ++k) d[k] = tag2(pv[k]) ^ tag;
    if (__all((((d[0] | d[1]) | (d[2] | d[3])) | ((d[4] | d[5]) | d[6])) == 0)) break;
#pragma unroll
    for (int k = 0; k < 7; ++k) have |= __all(d[k] == 0) ? 1u << k : 0u;
  }
#define WO(k) (w7[k] * f32x2{__uint_as_float(pv[k][0] & 0xffffff00u), __uint_as_float(pv[k][1] & 0xffffff00u)})
  o = ((o + WO(0)) + (WO(1) + WO(2))) + ((WO(3) + WO(4)) + (WO(5) + WO(6)));
#undef WO
  const u32 opk = cvtpk(o[0], o[1]);
  f32x4 acc[9];
#pragma unroll
  for (int t = 0; t < 9; ++t) acc[t] = f32x4{0.f, 0.f, 0.f, 0.f};
#define KST(k) { const u32x4 A_ = {dppu<BCAST + 4 * k>(opk), dppu<BCAST + 4 * k + 1>(opk), dppu<BCAST + 4 * k + 2>(opk), dppu<BCAST + 4 * k + 3>(opk)}; \
    _Pragma("unroll") for (int t = 0; t < 9; ++t) \
      acc[t] = __builtin_amdgcn_mfma_f32_16x16x32_bf16(__builtin_bit_cast(bf16x8, A_), __builtin_bit_cast(bf16x8, Bf[t][k]), acc[t], 0, 0, 0); }
  KST(0) KST(1) KST(2) KST(3)
#undef KST
#pragma unroll
  for (int i = 0; i < 2; ++i) {
    const float ylo = (r & 1) ? acc[4 * i + 1][0] : acc[4 * i][0], yhi = (r & 1) ? acc[4 * i + 3][0] : acc[4 * i + 2][0];
    put(ya[i], fg[i] | (r24((r & 2) ? yhi : ylo) & 0xffffff00u));
  }
  if (lane < 16) put(ya[2], fg[2] | (r24(acc[8][0]) & 0xffffff00u));
  if (wv >= nunit) return;
  u64 y[4];
  if (!poll<4>({yi, yi + 64, yi + 128, yi + 192}, ey, y)) return;
  float as = (ga(y[0]) + ga(y[1])) + (ga(y[2]) + ga(y[3])), fs = (gb(y[0]) + gb(y[1])) + (gb(y[2]) + gb(y[3]));
  as += dppf<XOR8>(as); fs += dppf<XOR8>(fs);
  float t = fold16(as, fs);
  t = rbf(fold32(t, t));
  auto sw = __builtin_amdgcn_permlane16_swap(__float_as_uint(t), __float_as_uint(t), false, false);
  const float res = rbf(xres + __uint_as_float(sw[0])) + __uint_as_float(sw[1]);
  if (lane < 8) *op = (u16)cvtpk(res, 0.f);
}

extern "C" void kernel_launch(void* const* d_in, const int* in_sizes, int n_in, void* d_out, int out_size, void* d_ws, size_t ws_size,
                              hipStream_t stream) {
  fused_decode<<<NWG, 512, 0, stream>>>((const char*)d_in[0], (const char*)d_in[1], (const char*)d_in[2], (const char*)d_in[3],
                                        (const char*)d_in[4], (const char*)d_in[6], (const char*)d_in[7], (const char*)d_in[8],
                                        (u16*)d_out, (char*)d_ws);
}
```
